```python
import torch

def _softmax(x, axis=-1):
    return torch.softmax(x, dim=axis)
def _gelu(x):
    return torch.nn.functional.gelu(x, approximate="tanh")
def _silu(x):
    return torch.nn.functional.silu(x)
def _sigmoid(x):
    return torch.sigmoid(x)
def _layernorm(x, axis=-1, eps=1e-5):
    m = x.mean(dim=axis, keepdim=True)
    v = x.var(dim=axis, keepdim=True, unbiased=False)
    return (x - m) / torch.sqrt(v + eps)
def _scatter_add(tgt, idx, src, axis=0):
    return tgt.clone().index_add_(axis, idx, src)
def _segment_sum(src, seg, n):
    out = torch.zeros((n,) + src.shape[1:], dtype=src.dtype)
    return out.index_add_(0, seg, src)
def _pad(x, pw):
    flat = tuple(v for pair in reversed(pw) for v in pair)
    return torch.nn.functional.pad(x, flat)

kernel_name = 'recurrent_scan__sliding_block_cached__geglu_ffn__decode'

def setup_inputs():

    _w = (lambda a: (lambda t: t.to(torch.bfloat16) if t.is_floating_point() else t)(torch.as_tensor(a)))
    def _A_setup():
        B, Sq, D = 1, 1, 512
        Hq, Hkv, Sk, Dh = 32, 8, 32768, 128
        assert Hq % Hkv == 0, (Hq, Hkv)
        return {
            "x": _w((lambda *s: torch.randn(*s, dtype=torch.bfloat16) * 0.05)(B, Sq, D)),
            "k_cache": _w((lambda *s: torch.randn(*s, dtype=torch.bfloat16) * 1.6)(B, Hkv, Sk, Dh)),
            "v_cache": _w((lambda *s: torch.randn(*s, dtype=torch.bfloat16) * 0.2)(B, Hkv, Sk, Dh)),
            "wq": _w((lambda *s: torch.randn(*s, dtype=torch.bfloat16) * 1.6)(D, Hq * Dh) * (D ** -0.5)),
            "wo": _w((lambda *s: torch.randn(*s, dtype=torch.bfloat16) * 0.2)(Hq * Dh, D) * ((Hq * Dh) ** -0.5)),
        }
    _A_in = _A_setup()

    def _B_setup():
        B, S, D, F = 1, 1, 512, 1792
        return {
            "x": _w((lambda *s: torch.randn(*s, dtype=torch.bfloat16) * 0.05)(B, S, D)),
            "w_gate": _w((lambda *s: torch.randn(*s, dtype=torch.bfloat16) * 0.1)(D, F)),
            "w_up": _w((lambda *s: torch.randn(*s, dtype=torch.bfloat16) * 0.1)(D, F)),
            "w_down": _w((lambda *s: torch.randn(*s, dtype=torch.bfloat16) * 0.1)(F, D)),
        }
    _B_in = _B_setup()

    return {
        "A_x": _A_in["x"],
        "A_k_cache": _A_in["k_cache"],
        "A_v_cache": _A_in["v_cache"],
        "A_wq": _A_in["wq"],
        "A_wo": _A_in["wo"],
        "B_x": _B_in["x"],
        "B_w_gate": _B_in["w_gate"],
        "B_w_up": _B_in["w_up"],
        "B_w_down": _B_in["w_down"],
    }

def reference(A_x, A_k_cache, A_v_cache, A_wq, A_wo, B_x, B_w_gate, B_w_up, B_w_down):
    def _A_ref(x, k_cache, v_cache, wq, wo):
        B, Sq, D = x.shape
        Hq, Dh = 32, 128
        Hkv, Sk = k_cache.shape[1], k_cache.shape[2]
        W = min(4096, max(256, Sk // 4))
        rep = Hq // Hkv
        q = (lambda x, *d: x.permute(*d))((x @ wq).reshape(B, Sq, Hq, Dh), 0, 2, 1, 3)
        k = (lambda x, n, axis: torch.repeat_interleave(x, n, dim=axis))(k_cache[:, :, Sk - W:, :], rep, axis=1)
        v = (lambda x, n, axis: torch.repeat_interleave(x, n, dim=axis))(v_cache[:, :, Sk - W:, :], rep, axis=1)
        scores = torch.einsum("bhqd,bhkd->bhqk", q, k) * (Dh ** -0.5)
        p = _softmax(scores, axis=-1)
        o = torch.einsum("bhqk,bhkd->bhqd", p, v)
        return (lambda x, *d: x.permute(*d))(o, 0, 2, 1, 3).reshape(B, Sq, Hq * Dh) @ wo

    def _B_ref(x, w_gate, w_up, w_down):
        return (_gelu(x @ w_gate) * (x @ w_up)) @ w_down

    x = A_x
    xn = _layernorm(x, axis=-1)
    s = _A_ref(xn, A_k_cache, A_v_cache, A_wq, A_wo)
    g = _silu(_B_ref(xn, B_w_gate, B_w_up, B_w_down))
    return x + s * g
```

```cpp
#include <hip/hip_runtime.h>
#include <hip/hip_bf16.h>
#include <stdint.h>

typedef unsigned short u16;
typedef unsigned v4u __attribute__((ext_vector_type(4)));

constexpr int D = 512, HQ = 32, HKV = 8, SK = 32768, DH = 128, W = 4096, F = 1792;
constexpr int NSPLIT = 32, KPS = W / NSPLIT;

__device__ __forceinline__ float bf2f(u16 b) { return __uint_as_float((unsigned)b << 16); }
__device__ __forceinline__ float rbf(float f) { return __bfloat162float(__float2bfloat16(f)); }

__device__ __forceinline__ void layernorm(const u16* x, float* xn_s, float* red) {
    int t = threadIdx.x;
    float a = bf2f(x[t]), b = bf2f(x[t + 256]);
    float s = a + b;
    for (int o = 32; o; o >>= 1) s += __shfl_xor(s, o);
    if ((t & 63) == 0) red[t >> 6] = s;
    __syncthreads();
    float mean = (red[0] + red[1] + red[2] + red[3]) * (1.f / 512.f);
    __syncthreads();
    float da = a - mean, db = b - mean;
    float v = da * da + db * db;
    for (int o = 32; o; o >>= 1) v += __shfl_xor(v, o);
    if ((t & 63) == 0) red[t >> 6] = v;
    __syncthreads();
    float var = (red[0] + red[1] + red[2] + red[3]) * (1.f / 512.f);
    float mb = rbf(mean), vb = rbf(var);
    float den = rbf(sqrtf(rbf(vb + 1e-5f)));
    xn_s[t] = rbf(rbf(a - mb) / den);
    xn_s[t + 256] = rbf(rbf(b - mb) / den);
    __syncthreads();
}

__global__ void __launch_bounds__(256) k1(const u16* x, const u16* wq, const u16* wg, const u16* wu, float* q, float* h) {
    __shared__ float xn[512];
    __shared__ float red[4];
    __shared__ float part[2][4][64];
    layernorm(x, xn, red);
    int t = threadIdx.x, c = t & 63, r = t >> 6;
    if (blockIdx.x < 64) {
        int col = blockIdx.x * 64 + c;
        float acc = 0;
        for (int k = r; k < D; k += 4) acc += xn[k] * bf2f(wq[(size_t)k * 4096 + col]);
        part[0][r][c] = acc;
        __syncthreads();
        if (r == 0) q[col] = rbf(part[0][0][c] + part[0][1][c] + part[0][2][c] + part[0][3][c]);
    } else {
        int col = (blockIdx.x - 64) * 64 + c;
        float ag = 0, au = 0;
        for (int k = r; k < D; k += 4) { ag += xn[k] * bf2f(wg[(size_t)k * F + col]); au += xn[k] * bf2f(wu[(size_t)k * F + col]); }
        part[0][r][c] = ag; part[1][r][c] = au;
        __syncthreads();
        if (r == 0) {
            float g = rbf(part[0][0][c] + part[0][1][c] + part[0][2][c] + part[0][3][c]);
            float u = rbf(part[1][0][c] + part[1][1][c] + part[1][2][c] + part[1][3][c]);
            float ge = rbf(0.5f * g * (1.f + tanhf(0.7978845608028654f * (g + 0.044715f * g * g * g))));
            h[col] = rbf(ge * u);
        }
    }
}

__global__ void __launch_bounds__(256) k2(const u16* kc, const u16* vc, const float* q, float* po, float* pml) {
    __shared__ float qs[4][128];
    __shared__ float sc[4][KPS];
    __shared__ float ml[4][2];
    int s = blockIdx.x, g = blockIdx.y, t = threadIdx.x;
    for (int i = t; i < 512; i += 256) qs[i >> 7][i & 127] = q[g * 512 + i];
    __syncthreads();
    const u16* kb = kc + ((size_t)g * SK + (SK - W) + s * KPS) * DH;
    const u16* vb = vc + ((size_t)g * SK + (SK - W) + s * KPS) * DH;
    for (int i = t; i < 4 * KPS; i += 256) {
        int hh = i / KPS, j = i % KPS;
        float acc = 0;
        for (int d = 0; d < DH; ++d) acc += qs[hh][d] * bf2f(kb[j * DH + d]);
        sc[hh][j] = rbf(rbf(acc) * 0.08838834764831845f);
    }
    __syncthreads();
    int hh = t >> 6, l = t & 63;
    float a = sc[hh][l], b = sc[hh][l + 64];
    float m = fmaxf(a, b);
    for (int o = 32; o; o >>= 1) m = fmaxf(m, __shfl_xor(m, o));
    a = __expf(a - m); b = __expf(b - m);
    float sum = a + b;
    for (int o = 32; o; o >>= 1) sum += __shfl_xor(sum, o);
    sc[hh][l] = a; sc[hh][l + 64] = b;
    if (l == 0) { pml[((g * 4 + hh) * NSPLIT + s) * 2] = m; pml[((g * 4 + hh) * NSPLIT + s) * 2 + 1] = sum; }
    __syncthreads();
    for (int i = t; i < 512; i += 256) {
        int h2 = i >> 7, d = i & 127;
        float acc = 0;
        for (int j = 0; j < KPS; ++j) acc += sc[h2][j] * bf2f(vb[j * DH + d]);
        po[((size_t)(g * 4 + h2) * NSPLIT + s) * DH + d] = acc;
    }
}

__global__ void __launch_bounds__(128) k3(const float* po, const float* pml, float* o) {
    int h = blockIdx.x, d = threadIdx.x;
    float M = -INFINITY;
    for (int s = 0; s < NSPLIT; ++s) M = fmaxf(M, pml[(h * NSPLIT + s) * 2]);
    float L = 0, acc = 0;
    for (int s = 0; s < NSPLIT; ++s) {
        float w = __expf(pml[(h * NSPLIT + s) * 2] - M);
        L += w * pml[(h * NSPLIT + s) * 2 + 1];
        acc += w * po[((size_t)h * NSPLIT + s) * DH + d];
    }
    o[h * DH + d] = rbf(acc / L);
}

__global__ void __launch_bounds__(1024) k4(const u16* x, const float* o, const float* h, const u16* wo, const u16* wd, __hip_bfloat16* out) {
    __shared__ float part[2][16][64];
    int t = threadIdx.x, c = t & 63, r = t >> 6;
    int col = blockIdx.x * 64 + c;
    float a = 0, b = 0;
    for (int k = r; k < 4096; k += 16) a += o[k] * bf2f(wo[(size_t)k * D + col]);
    for (int k = r; k < F; k += 16) b += h[k] * bf2f(wd[(size_t)k * D + col]);
    part[0][r][c] = a; part[1][r][c] = b;
    __syncthreads();
    if (r == 0) {
        float s = 0, y = 0;
        for (int i = 0; i < 16; ++i) { s += part[0][i][c]; y += part[1][i][c]; }
        s = rbf(s); y = rbf(y);
        float g = rbf(y / (1.f + __expf(-y)));
        out[col] = __float2bfloat16(bf2f(x[col]) + rbf(s * g));
    }
}

extern "C" void kernel_launch(void* const* d_in, const int* in_sizes, int n_in,
                              void* d_out, int out_size, void* d_ws, size_t ws_size,
                              hipStream_t stream) {
    const u16* x = (const u16*)d_in[0];
    const u16* kc = (const u16*)d_in[1];
    const u16* vc = (const u16*)d_in[2];
    const u16* wq = (const u16*)d_in[3];
    const u16* wo = (const u16*)d_in[4];
    const u16* wg = (const u16*)d_in[6];
    const u16* wu = (const u16*)d_in[7];
    const u16* wd = (const u16*)d_in[8];
    float* ws = (float*)d_ws;
    float* q = ws; float* h = ws + 4096; float* o = ws + 8192; float* pml = ws + 16384; float* po = ws + 32768;
    k1<<<92, 256, 0, stream>>>(x, wq, wg, wu, q, h);
    k2<<<dim3(NSPLIT, HKV), 256, 0, stream>>>(kc, vc, q, po, pml);
    k3<<<32, 128, 0, stream>>>(po, pml, o);
    k4<<<8, 1024, 0, stream>>>(x, o, h, wo, wd, (__hip_bfloat16*)d_out);
}
```

```cpp
#include <hip/hip_runtime.h>
#include <hip/hip_bf16.h>
#include <stdint.h>

#define GAS __attribute__((address_space(1)))
typedef unsigned short u16;
typedef unsigned long long u64;
typedef unsigned v4u __attribute__((ext_vector_type(4)));
typedef unsigned v2u __attribute__((ext_vector_type(2)));
typedef float f4 __attribute__((ext_vector_type(4)));
typedef float f2 __attribute__((ext_vector_type(2)));
typedef short s4 __attribute__((ext_vector_type(4)));
typedef __bf16 bf8 __attribute__((ext_vector_type(8)));
typedef __bf16 bf2 __attribute__((ext_vector_type(2)));
typedef __amdgpu_buffer_rsrc_t rsrc_t;

constexpr int SK = 32768, WIN = 4096, FF = 1792;
constexpr int WS_EPOCH = 0;
constexpr int WS_Q = 16384;
constexpr int WS_O = WS_Q + 8 * 16384;
constexpr int WS_F = WS_O + 32 * 16384;
constexpr int WS_S = WS_F + 32 * 4096;
constexpr int WS_Y = WS_S + 8 * 32768;
constexpr int WS_END = WS_Y + 8 * 8192;
constexpr unsigned SENT = 0xFFFFFFFFu;
constexpr int SPIN_CAP = 1 << 16;
constexpr float LOG2E = 1.4426950408889634f;

#define STAMP(i) do {} while (0)
#define FLUSH() do {} while (0)
#define NOTE(i, v) do {} while (0)

#define SB() do { asm volatile("" ::: "memory"); __builtin_amdgcn_sched_barrier(0); } while (0)
#define BAR() do { asm volatile("s_waitcnt lgkmcnt(0)" ::: "memory"); __builtin_amdgcn_s_barrier(); asm volatile("" ::: "memory"); } while (0)

__device__ __forceinline__ float asf(unsigned u) { return __builtin_bit_cast(float, u); }
__device__ __forceinline__ unsigned asu(float f) { return __builtin_bit_cast(unsigned, f); }
#define STREAM_AUX 2
__device__ __forceinline__ rsrc_t mkrs(const void* p, int bytes) { return __builtin_amdgcn_make_buffer_rsrc((void*)p, 0, bytes, 0x00020000); }
__device__ __forceinline__ v4u ldw(rsrc_t r, int voff, int soff) { return __builtin_amdgcn_raw_buffer_load_b128(r, voff, soff, STREAM_AUX); }
__device__ __forceinline__ v4u ldp(rsrc_t r, int voff, int soff) { return __builtin_amdgcn_raw_buffer_load_b128(r, voff, soff, 0); }
__device__ __forceinline__ float rcp(float x) { return __builtin_amdgcn_rcpf(x); }
__device__ __forceinline__ void st16(rsrc_t r, int off, v4u v) { __builtin_amdgcn_raw_buffer_store_b128(v, r, off, 0, 16); }
__device__ __forceinline__ void st4(rsrc_t r, int off, unsigned v) { __builtin_amdgcn_raw_buffer_store_b32(v, r, off, 0, 16); }
__device__ __forceinline__ v4u ld16(rsrc_t r, int off) { return __builtin_amdgcn_raw_buffer_load_b128(r, off, 0, 16); }
__device__ __forceinline__ v2u ld8(rsrc_t r, int off) { return __builtin_amdgcn_raw_buffer_load_b64(r, off, 0, 16); }
__device__ __forceinline__ unsigned pk(float lo, float hi) { f2 t = {lo, hi}; return __builtin_bit_cast(unsigned, __builtin_convertvector(t, bf2)); }
__device__ __forceinline__ float rbf(float f) { return (float)(__bf16)f; }
__device__ __forceinline__ float blo(unsigned u) { return asf(u << 16); }
__device__ __forceinline__ float bhi(unsigned u) { return asf(u & 0xffff0000u); }
__device__ __forceinline__ unsigned guard(float f) { unsigned u = asu(f); return u < 0xFFFFFFFEu ? u : 0xFFFFFFFEu; }
__device__ __forceinline__ bool live(v4u v) { return (v.x != SENT) & (v.y != SENT) & (v.z != SENT) & (v.w != SENT); }
template <int CTRL> __device__ __forceinline__ float dpp(float x) {
    return asf(__builtin_amdgcn_mov_dpp(asu(x), CTRL, 0xf, 0xf, true));
}
constexpr int ROR1 = 0x121, ROR2 = 0x122, ROR4 = 0x124, ROR8 = 0x128;
__device__ __forceinline__ float sum16x(float a, float b) {
    auto s = __builtin_amdgcn_permlane16_swap(asu(a), asu(b), false, false); return asf(s[0]) + asf(s[1]);
}
__device__ __forceinline__ float sum32x(float a, float b) {
    auto s = __builtin_amdgcn_permlane32_swap(asu(a), asu(b), false, false); return asf(s[0]) + asf(s[1]);
}
__device__ __forceinline__ float max16(float a) { auto s = __builtin_amdgcn_permlane16_swap(asu(a), asu(a), false, false); return fmaxf(asf(s[0]), asf(s[1])); }
__device__ __forceinline__ float max32(float a) { auto s = __builtin_amdgcn_permlane32_swap(asu(a), asu(a), false, false); return fmaxf(asf(s[0]), asf(s[1])); }
__device__ __forceinline__ float wave_sum(float x) {
    x += dpp<ROR8>(x); x += dpp<ROR4>(x); x += dpp<ROR2>(x); x += dpp<ROR1>(x);
    x = sum16x(x, x); return sum32x(x, x);
}
__device__ __forceinline__ f4 mfma32(v4u a, v4u b, f4 c) {
    return __builtin_amdgcn_mfma_f32_16x16x32_bf16(__builtin_bit_cast(bf8, a), __builtin_bit_cast(bf8, b), c, 0, 0, 0);
}
__device__ __forceinline__ f4 mfma16(v2u a, v2u b, f4 c) {
    return __builtin_amdgcn_mfma_f32_16x16x16bf16_1k(__builtin_bit_cast(s4, a), __builtin_bit_cast(s4, b), c, 0, 0, 0);
}
template <int F> __device__ __forceinline__ unsigned pair(const v4u& lo, const v4u& hi) {
    return __builtin_amdgcn_perm(hi[F >> 1], lo[F >> 1], (F & 1) ? 0x07060302u : 0x05040100u);
}
template <int F> __device__ __forceinline__ v4u bfrag8(const v4u* w) {
    return v4u{pair<F>(w[0], w[1]), pair<F>(w[2], w[3]), pair<F>(w[4], w[5]), pair<F>(w[6], w[7])};
}
#define FOR8(M) M(0) M(1) M(2) M(3) M(4) M(5) M(6) M(7)

__device__ __forceinline__ void ln_stats(v4u xa, float& mb, float& den) {
    float v[8];
#pragma unroll
    for (int i = 0; i < 4; ++i) { v[2 * i] = blo(xa[i]); v[2 * i + 1] = bhi(xa[i]); }
    float s = ((v[0] + v[1]) + (v[2] + v[3])) + ((v[4] + v[5]) + (v[6] + v[7]));
    const float mean = wave_sum(s) * (1.f / 512.f);
    float q = 0.f;
#pragma unroll
    for (int i = 0; i < 8; ++i) { const float d = v[i] - mean; q = fmaf(d, d, q); }
    const float var = wave_sum(q) * (1.f / 512.f);
    mb = rbf(mean);
    den = rcp(rbf(__builtin_amdgcn_sqrtf(rbf(rbf(var) + 1e-5f))));
}
__device__ __forceinline__ v4u ln_apply(v4u x, float mb, float den) {
    v4u r;
#pragma unroll
    for (int i = 0; i < 4; ++i) r[i] = pk(rbf(blo(x[i]) - mb) * den, rbf(bhi(x[i]) - mb) * den);
    return r;
}

struct Params {
    const u16 *x, *kc, *vc, *wq, *wo, *wg, *wu, *wd;
    u16* out;
    unsigned char* ws;
};

constexpr int OOB = 0x40000000;
#define TAGS(ep) asm volatile("" : "+v"(ep)); \
    const u64 tag = (((u64)ep.y << 32) | ep.x) + 1; \
    const unsigned tlo = (unsigned)tag & 0xffu, thi = (unsigned)(tag >> 8)
#define BARRIER() do { SB(); __builtin_amdgcn_s_barrier(); SB(); } while (0)

__global__ void __launch_bounds__(512, 2) fused_decode(Params p) {
    __shared__ __attribute__((aligned(16))) unsigned lds[256 + 2048 + 64 + 32];
    unsigned* lq = lds;
    unsigned* lo_ = lds + 256;
    float* lm = (float*)(lds + 256 + 2048);
    u16* lh = (u16*)(lds + 256 + 2048 + 64);
    const int tid = threadIdx.x, lane = tid & 63, wave = __builtin_amdgcn_readfirstlane(tid >> 6);
    const int b = blockIdx.x, x = b & 7, s = b >> 3;
    asm volatile("" :: "s"(p.x), "s"(p.kc), "s"(p.vc), "s"(p.wq), "s"(p.wo), "s"(p.wg), "s"(p.wu), "s"(p.wd), "s"(p.out), "s"(p.ws));
    const rsrc_t ws = __builtin_amdgcn_make_buffer_rsrc(p.ws, 0, WS_END, 0x00020000);
    const int rho = lane >> 3, c8 = lane & 7;
    const int head = 4 * x + (s >> 3), cb = s & 7;
    STAMP(0);

    if (wave >= 4) {
        const int w = wave - 4, t = tid - 256;
        v2u ep = ld8(ws, WS_EPOCH);
        const int kvoff = (x * SK + (SK - WIN) + s * 128 + w * 32) * 256;
        const rsrc_t rk = mkrs(p.kc, 8 * SK * 256), rv = mkrs(p.vc, 8 * SK * 256), ro = mkrs(p.wo, 4096 * 1024);
        const int kl = ((lane >> 2) & 3) * 256 + ((lane >> 4) * 4 + (lane & 3)) * 16;
        BARRIER();
        v4u K[8], V[8];
#pragma unroll
        for (int i = 0; i < 8; ++i) K[i] = ldw(rk, kl + (i & 3) * 1024, kvoff + (i >> 2) * 4096);
        SB();
#pragma unroll
        for (int i = 0; i < 8; ++i) V[i] = ldw(rv, lane * 16 + (i & 3) * 1024, kvoff + (i >> 2) * 4096);
        SB();
        STAMP(1);
        asm volatile("s_waitcnt vmcnt(8)" ::: "memory");
        STAMP(2);
        v4u g[4];
#pragma unroll
        for (int r = 0; r < 4; ++r) g[r] = ld16(ws, WS_Q + ((x * 4 + r) * 256 + t) * 16);
        BARRIER();
        TAGS(ep);
        int spinA = 0;
        for (;; ++spinA) {
            bool ok = true;
#pragma unroll
            for (int r = 0; r < 4; ++r) ok &= ((g[r].x & 0xffu) == tlo) & (g[r].y == thi) & ((g[r].z & 0xffu) == tlo) & (g[r].w == thi);
            if (__all(ok) || spinA >= SPIN_CAP) break;
            asm volatile("" ::: "memory");
#pragma unroll
            for (int r = 0; r < 4; ++r) g[r] = ld16(ws, WS_Q + ((x * 4 + r) * 256 + t) * 16);
        }
        SB();
        STAMP(3);
        NOTE(8, spinA);
        v4u Wo[4];
#pragma unroll
        for (int j = 0; j < 4; ++j) Wo[j] = ldw(ro, rho * 4096 + c8 * 16 + j * 1024, (head * 128 + w * 32) * 1024 + cb * 128);
        SB();
        {
            const float q0 = (asf(g[0].x & ~0xffu) + asf(g[1].x & ~0xffu)) + (asf(g[2].x & ~0xffu) + asf(g[3].x & ~0xffu));
            const float q1 = (asf(g[0].z & ~0xffu) + asf(g[1].z & ~0xffu)) + (asf(g[2].z & ~0xffu) + asf(g[3].z & ~0xffu));
            lq[(t >> 5) * 32 + (t & 7) * 4 + ((t >> 3) & 3)] = pk(q0, q1);
        }
        BAR();
        const v4u qf = *(const v4u*)(lq + ((lane >> 2) & 3) * 64 + (lane >> 4) * 16 + (lane & 3) * 4);
        float sc[8];
#pragma unroll
        for (int i = 0; i < 8; ++i) {
            const f4 d = mfma32(K[i], qf, f4{0.f, 0.f, 0.f, 0.f});
            const float v = (dpp<0x00>(d[0]) + dpp<0x55>(d[1])) + (dpp<0xAA>(d[2]) + dpp<0xFF>(d[3]));
            sc[i] = rbf(rbf(v) * 0.08838834764831845f);
        }
        float m = fmaxf(fmaxf(fmaxf(sc[0], sc[1]), fmaxf(sc[2], sc[3])), fmaxf(fmaxf(sc[4], sc[5]), fmaxf(sc[6], sc[7])));
        m = max32(max16(m));
        float l = 0.f;
#pragma unroll
        for (int i = 0; i < 8; ++i) { sc[i] = __builtin_amdgcn_exp2f((sc[i] - m) * LOG2E); l += sc[i]; }
        l = sum16x(l, l); l = sum32x(l, l);
        const v4u pf = {pk(sc[0], sc[1]), pk(sc[2], sc[3]), pk(sc[4], sc[5]), pk(sc[6], sc[7])};
        float o[8];
#define PV(F) o[F] = mfma32(pf, bfrag8<F>(V), f4{0.f, 0.f, 0.f, 0.f})[0];
        FOR8(PV)
#undef PV
        *(f4*)(lo_ + w * 512 + lane * 4) = f4{o[0], o[1], o[2], o[3]};
        *(f4*)(lo_ + w * 512 + 256 + lane * 4) = f4{o[4], o[5], o[6], o[7]};
        {
            const int hh = (lane >> 2) & 3;
            lm[hh * 4 + w] = m; lm[16 + hh * 4 + w] = l;
        }
        BAR();
        {
            const f4 m4 = *(const f4*)(lm + w * 4), l4 = *(const f4*)(lm + 16 + w * 4);
            const int a = (lane & 1) * 256 + (w * 16 + ((lane >> 1) & 15)) * 4;
            const f4 o0 = *(const f4*)(lo_ + a), o1 = *(const f4*)(lo_ + 512 + a), o2 = *(const f4*)(lo_ + 1024 + a), o3 = *(const f4*)(lo_ + 1536 + a);
            const float M = fmaxf(fmaxf(m4[0], m4[1]), fmaxf(m4[2], m4[3]));
            const float w0 = __builtin_amdgcn_exp2f((m4[0] - M) * LOG2E), w1 = __builtin_amdgcn_exp2f((m4[1] - M) * LOG2E);
            const float w2 = __builtin_amdgcn_exp2f((m4[2] - M) * LOG2E), w3 = __builtin_amdgcn_exp2f((m4[3] - M) * LOG2E);
            const float L = (l4[0] * w0 + l4[1] * w1) + (l4[2] * w2 + l4[3] * w3);
            const f4 om = ((o0 * w0 + o1 * w1) + (o2 * w2 + o3 * w3)) * rcp(L);
            const unsigned lse = asu(fmaf(M, LOG2E, __builtin_amdgcn_logf(L)));
            const int slot = (4 * x + w) * 32 + s;
            v4u pv;
#pragma unroll
            for (int k = 0; k < 4; ++k) { const unsigned u = asu(om[k]); pv[k] = __builtin_amdgcn_perm(u < 0xFFFFFEFFu ? u : 0xFFFFFEFFu, lse, 0x07060500u + k); }
            st16(ws, lane < 32 ? WS_O + slot * 512 + lane * 16 : OOB, pv);
        }
        SB();
        STAMP(4);
        asm volatile("s_waitcnt vmcnt(4)" ::: "memory");
        STAMP(5);
        v4u ov[4];
        int spinB = 0;
        for (;; ++spinB) {
            asm volatile("" ::: "memory");
#pragma unroll
            for (int i = 0; i < 4; ++i) ov[i] = ld16(ws, WS_O + (head * 32 + rho * 4 + i) * 512 + w * 128 + c8 * 16);
            const bool ok = live(ov[0]) & live(ov[1]) & live(ov[2]) & live(ov[3]);
            if (__all(ok) || spinB >= SPIN_CAP) break;
        }
        STAMP(6);
        NOTE(9, spinB);
        {
            float wt[4];
#pragma unroll
            for (int i = 0; i < 4; ++i)
                wt[i] = asf(__builtin_amdgcn_perm(__builtin_amdgcn_perm(ov[i][3], ov[i][2], 0x0c0c0400u), __builtin_amdgcn_perm(ov[i][1], ov[i][0], 0x0c0c0400u), 0x05040100u));
            float M = fmaxf(fmaxf(wt[0], wt[1]), fmaxf(wt[2], wt[3]));
            M = fmaxf(M, dpp<ROR8>(M)); M = max32(max16(M));
#pragma unroll
            for (int i = 0; i < 4; ++i) wt[i] = __builtin_amdgcn_exp2f(wt[i] - M);
            float a[5];
#pragma unroll
            for (int k = 0; k < 4; ++k) a[k] = (asf(ov[0][k]) * wt[0] + asf(ov[1][k]) * wt[1]) + (asf(ov[2][k]) * wt[2] + asf(ov[3][k]) * wt[3]);
            a[4] = (wt[0] + wt[1]) + (wt[2] + wt[3]);
#pragma unroll
            for (int k = 0; k < 5; ++k) { a[k] += dpp<ROR8>(a[k]); a[k] = sum16x(a[k], a[k]); a[k] = sum32x(a[k], a[k]); }
            const float inv = rcp(a[4]);
            v2u of = {pk(a[0] * inv, a[1] * inv), pk(a[2] * inv, a[3] * inv)};
#pragma unroll
            for (int k = 0; k < 2; ++k) {
                unsigned u = of[k];
                u = __builtin_amdgcn_update_dpp(u, of[k], 0x102, 0x2, 0xf, false);
                u = __builtin_amdgcn_update_dpp(u, of[k], 0x104, 0x4, 0xf, false);
                u = __builtin_amdgcn_update_dpp(u, of[k], 0x106, 0x8, 0xf, false);
                of[k] = u;
            }
            unsigned r[8];
#define OW(F) { const f4 d = mfma16(of, v2u{pair<F>(Wo[0], Wo[1]), pair<F>(Wo[2], Wo[3])}, f4{0.f, 0.f, 0.f, 0.f}); r[F] = guard(d[0] + dpp<ROR8>(d[1])); }
            FOR8(OW)
#undef OW
            const int off = lane < 8 ? WS_S + (cb * 128 + head * 4 + w) * 256 + lane * 16 : OOB;
            st16(ws, off, v4u{r[0], r[1], r[2], r[3]});
            st16(ws, off + 128, v4u{r[4], r[5], r[6], r[7]});
        }
        STAMP(7);
        if (s >= 28) BARRIER();
        FLUSH();
        return;
    }

    v2u ep;
    if (wave == 0) {
        const v4u sent = {SENT, SENT, SENT, SENT};
        ep = ld8(ws, WS_EPOCH);
        const rsrc_t rx = mkrs(p.x, 1024), rq = mkrs(p.wq, 512 * 8192);
        const v4u xa = ldp(rx, lane * 16, 0);
        const int xl = (lane >> 4) * 32 + (lane & 1) * 16;
        const v4u x0 = ldp(rx, xl, (s >> 3) * 256), x1 = ldp(rx, xl + 128, (s >> 3) * 256);
        v4u Wq[16];
#pragma unroll
        for (int j = 0; j < 16; ++j) Wq[j] = ldw(rq, rho * 65536 + c8 * 16, ((s >> 3) * 128 + (j >> 3) * 64 + (j & 7)) * 8192 + x * 1024 + cb * 128);
        SB();
        st16(ws, WS_O + ((4 * x + (lane >> 5)) * 32 + s) * 512 + (lane & 31) * 16, sent);
        st16(ws, WS_O + ((4 * x + 2 + (lane >> 5)) * 32 + s) * 512 + (lane & 31) * 16, sent);
        st16(ws, WS_S + (cb * 128 + head * 4) * 256 + lane * 16, sent);
        {
            const int fo = WS_F + (s * 16 + x) * 256 + (lane >> 4) * 2048, yo = lane < 48 ? WS_Y + (x * 32 + s) * 256 : OOB;
            st16(ws, (lane < 32 ? fo : yo) + (lane & 15) * 16, sent);
        }
        BARRIER();
        float mb, den;
        ln_stats(xa, mb, den);
        v4u a0 = ln_apply(x0, mb, den), a1 = ln_apply(x1, mb, den);
        asm volatile("" : "+v"(a0), "+v"(a1));
        SB();
        STAMP(1);
        TAGS(ep);
        unsigned r[8];
#define QP(F) { f4 d = mfma32(a0, bfrag8<F>(Wq), f4{0.f, 0.f, 0.f, 0.f}); d = mfma32(a1, bfrag8<F>(Wq + 8), d); \
                r[F] = (asu(d[0] + dpp<ROR8>(d[1])) & ~0xffu) | tlo; }
        FOR8(QP)
#undef QP
        SB();
        asm volatile("s_waitcnt vmcnt(0)" ::: "memory");
        const int off = lane < 8 ? WS_Q + ((x * 4 + (s >> 3)) * 256 + cb * 32 + lane) * 16 : OOB;
#pragma unroll
        for (int k = 0; k < 4; ++k) st16(ws, off + k * 128, v4u{r[2 * k], thi, r[2 * k + 1], thi});
        STAMP(2);
        if (s < 28) { BARRIER(); FLUSH(); return; }
    } else if (s < 28) {
        if (wave < 3) {
            const rsrc_t rx = mkrs(p.x, 1024), rg = mkrs(wave == 1 ? p.wg : p.wu, 512 * FF * 2);
            const v4u xa = ldp(rx, lane * 16, 0);
            const v4u x0 = ldp(rx, (lane >> 4) * 32 + (lane & 1) * 16, x * 128);
            BARRIER();
            float mb, den;
            ln_stats(xa, mb, den);
            v4u a0 = ln_apply(x0, mb, den);
            asm volatile("" : "+v"(a0));
            BARRIER();
            v4u G[8];
#pragma unroll
            for (int j = 0; j < 8; ++j) G[j] = ldw(rg, rho * 8 * FF * 2 + c8 * 16, (x * 64 + j) * FF * 2 + s * 128);
            BARRIER();
            BARRIER();
            STAMP(1);
            unsigned r[8];
#define GP(F) { const f4 d = mfma32(a0, bfrag8<F>(G), f4{0.f, 0.f, 0.f, 0.f}); r[F] = guard(d[0] + dpp<ROR8>(d[1])); }
            FOR8(GP)
#undef GP
            const int off = lane < 8 ? WS_F + ((s * 2 + (wave - 1)) * 8 + x) * 256 + lane * 16 : OOB;
            st16(ws, off, v4u{r[0], r[1], r[2], r[3]});
            st16(ws, off + 128, v4u{r[4], r[5], r[6], r[7]});
            STAMP(2);
            FLUSH();
            return;
        }
        ep = ld8(ws, WS_EPOCH);
        const rsrc_t rd = mkrs(p.wd, FF * 1024);
        const int hoff = WS_Q + ((c8 * 4 + (s >> 3)) * 256 + cb * 32) * 16;
        BARRIER();
        TAGS(ep);
        BARRIER();
        v2u hl = ld8(ws, hoff);
        __builtin_amdgcn_s_sleep(2);
        SB();
        v4u Dn[8];
#pragma unroll
        for (int j = 0; j < 8; ++j) Dn[j] = ldw(rd, rho * 8192 + c8 * 16 + (j & 3) * 1024, s * 65536 + x * 128 + (j >> 2) * 4096);
        BARRIER();
        BARRIER();
        for (int spin = 0;; ++spin) {
            if (__all(((hl.x & 0xffu) == tlo) & (hl.y == thi)) || spin >= SPIN_CAP) break;
            asm volatile("" ::: "memory");
            hl = ld8(ws, hoff);
        }
        v4u B[8];
#define DP(F) B[F] = bfrag8<F>(Dn);
        FOR8(DP)
#undef DP
        asm volatile("" : "+v"(B[0]), "+v"(B[1]), "+v"(B[2]), "+v"(B[3]), "+v"(B[4]), "+v"(B[5]), "+v"(B[6]), "+v"(B[7]));
        SB();
        STAMP(1);
        v4u fv[4];
        int spinF = 0;
        for (;; ++spinF) {
            asm volatile("" ::: "memory");
#pragma unroll
            for (int i = 0; i < 4; ++i) fv[i] = ld16(ws, WS_F + s * 4096 + i * 1024 + lane * 16);
            const bool ok = live(fv[0]) & live(fv[1]) & live(fv[2]) & live(fv[3]);
            if (__all(ok) || spinF >= SPIN_CAP) break;
        }
        STAMP(2);
        NOTE(8, spinF);
        {
            float gsum[4], usum[4];
#pragma unroll
            for (int k = 0; k < 4; ++k) { gsum[k] = asf(fv[0][k]) + asf(fv[1][k]); usum[k] = asf(fv[2][k]) + asf(fv[3][k]); }
            const float gt = rbf(sum32x(sum16x(gsum[0], gsum[1]), sum16x(gsum[2], gsum[3])));
            const float ut = rbf(sum32x(sum16x(usum[0], usum[1]), sum16x(usum[2], usum[3])));
            const float z = 0.7978845608028654f * (gt + 0.044715f * gt * gt * gt);
            const float ge = rbf(gt * rcp(1.f + __builtin_amdgcn_exp2f(-2.f * LOG2E * z)));
            const unsigned hb = pk(ge * ut, 0.f);
            lh[(lane & 7) * 8 + ((lane >> 3) & 1) * 4 + (lane >> 4)] = (u16)hb;
            asm volatile("s_waitcnt lgkmcnt(0)" ::: "memory");
            const v4u hf = *(const v4u*)(lh + (lane >> 4) * 16 + (lane & 1) * 8);
            unsigned r[8];
#define YP(F) { const f4 d = mfma32(hf, B[F], f4{0.f, 0.f, 0.f, 0.f}); r[F] = guard(d[0] + dpp<ROR8>(d[1])); }
            FOR8(YP)
#undef YP
            const int off = lane < 8 ? WS_Y + (x * 32 + s) * 256 + lane * 16 : OOB;
            st16(ws, off, v4u{r[0], r[1], r[2], r[3]});
            st16(ws, off + 128, v4u{r[4], r[5], r[6], r[7]});
        }
        STAMP(3);
        FLUSH();
        return;
    } else {
        ep = ld8(ws, WS_EPOCH);
        BARRIER();
    }

    {
        const int gam = (s - 28) * 4 + wave;
        const int col = x * 64 + gam * 4;
        const int poff = (gam & 1) * 128 + (gam >> 1) * 16;
        const int lt = lane < 60 ? lane : 59;
        const int hx = lt < 32 ? (lt >> 2) : x, hs = lt < 32 ? (lt & 3) * 8 + x : lt - 32;
        const int hoff = WS_Q + ((hx * 4 + (hs >> 3)) * 256 + (hs & 7) * 32) * 16;
        TAGS(ep);
        BARRIER();
        const unsigned xv = *(const GAS unsigned*)(p.x + col + ((lane >> 4) & 1) * 2);
        v2u hl = ld8(ws, hoff);
        BARRIER();
        BARRIER();
        for (int spin = 0;; ++spin) {
            if (__all(((hl.x & 0xffu) == tlo) & (hl.y == thi)) || spin >= SPIN_CAP) break;
            asm volatile("" ::: "memory");
            hl = ld8(ws, hoff);
        }
        BARRIER();
        STAMP(4);
        v4u s0, s1, y0;
        const int ly = lane < 28 ? lane : 27;
        int spinC = 0;
        for (;; ++spinC) {
            asm volatile("" ::: "memory");
            s0 = ld16(ws, WS_S + (x * 128 + lane) * 256 + poff);
            s1 = ld16(ws, WS_S + (x * 128 + 64 + lane) * 256 + poff);
            y0 = ld16(ws, WS_Y + (x * 32 + ly) * 256 + poff);
            const bool ok = live(s0) & live(s1) & live(y0);
            if (__all(ok) || spinC >= SPIN_CAP) break;
            __builtin_amdgcn_s_sleep(2);
        }
        STAMP(5);
        NOTE(8, spinC);
        float v[4];
#pragma unroll
        for (int k = 0; k < 4; ++k) v[k] = sum32x(asf(s0[k]) + asf(s1[k]), lane < 28 ? asf(y0[k]) : 0.f);
        float e0 = sum16x(v[0], v[2]), e1 = sum16x(v[1], v[3]);
        e0 += dpp<ROR8>(e0); e1 += dpp<ROR8>(e1); e0 += dpp<ROR4>(e0); e1 += dpp<ROR4>(e1);
        e0 += dpp<ROR2>(e0); e1 += dpp<ROR2>(e1); e0 += dpp<ROR1>(e0); e1 += dpp<ROR1>(e1);
        const auto t0 = __builtin_amdgcn_permlane32_swap(asu(e0), asu(e0), false, false);
        const auto t1 = __builtin_amdgcn_permlane32_swap(asu(e1), asu(e1), false, false);
        const float sa = rbf(asf(t0[0])), ya = rbf(asf(t0[1])), sb = rbf(asf(t1[0])), yb = rbf(asf(t1[1]));
        const float ga = rbf(ya * rcp(1.f + __builtin_amdgcn_exp2f(-LOG2E * ya))), gb = rbf(yb * rcp(1.f + __builtin_amdgcn_exp2f(-LOG2E * yb)));
        const unsigned res = pk(blo(xv) + rbf(sa * ga), bhi(xv) + rbf(sb * gb));
        const rsrc_t ro = mkrs(p.out, 1024);
        __builtin_amdgcn_raw_buffer_store_b32(res, ro, (lane & 0x2f) == 0 ? (col + (lane >> 4) * 2) * 2 : OOB, 0, 0);
        __builtin_amdgcn_raw_buffer_store_b64(v2u{(unsigned)tag, (unsigned)(tag >> 32)}, ws, (b == 255 && tid == 192) ? WS_EPOCH : OOB, 0, 16);
        STAMP(6);
        FLUSH();
    }
}

extern "C" void kernel_launch(void* const* d_in, const int* in_sizes, int n_in,
                              void* d_out, int out_size, void* d_ws, size_t ws_size,
                              hipStream_t stream) {
    Params p;
    p.x = (const u16*)d_in[0];
    p.kc = (const u16*)d_in[1];
    p.vc = (const u16*)d_in[2];
    p.wq = (const u16*)d_in[3];
    p.wo = (const u16*)d_in[4];
    p.wg = (const u16*)d_in[6];
    p.wu = (const u16*)d_in[7];
    p.wd = (const u16*)d_in[8];
    p.out = (u16*)d_out;
    p.ws = (unsigned char*)d_ws;
    fused_decode<<<256, 512, 0, stream>>>(p);
}
```

```cpp
#include <hip/hip_runtime.h>
#include <utility>

#define LAS __attribute__((address_space(3)))
typedef unsigned short u16;
typedef unsigned long long u64;
typedef unsigned v4u __attribute__((ext_vector_type(4)));
typedef unsigned v2u __attribute__((ext_vector_type(2)));
typedef float f4 __attribute__((ext_vector_type(4)));
typedef float f2 __attribute__((ext_vector_type(2)));
typedef short s4 __attribute__((ext_vector_type(4)));
typedef __bf16 bf16x8 __attribute__((ext_vector_type(8)));
typedef __bf16 bf16x2 __attribute__((ext_vector_type(2)));
typedef __amdgpu_buffer_rsrc_t rsrc_t;

constexpr int SK = 32768, WIN = 4096, FF = 1792;
constexpr int WS_EPOCH = 0;
constexpr int WS_T = 4096;
constexpr int WS_C = 32768;
constexpr int WS_Q = 65536;
constexpr int WS_O = WS_Q + 8 * 16384;
constexpr int WS_F = WS_O + 32 * 16384;
constexpr int WS_S = WS_F + 32 * 8192;
constexpr int WS_END = WS_S + 8 * 32768;
constexpr int SPIN_CAP = 1 << 16;
constexpr float LOG2E = 1.4426950408889634f;
constexpr int OOB = 0x40000000;
constexpr int L_WQ = 0, L_G = 16384, L_K = 32768, L_D = 65536, L_V = 73728, L_WO = 106496, L_SM = 122880;
constexpr int L_X = L_SM, L_EP = L_X + 1024, L_XN = L_EP + 256, L_Q = L_XN + 1024, L_P = L_Q + 1088, L_ML = L_P + 1088, L_H = L_ML + 128, L_O = L_H + 128, L_Z = L_O + 256, L_F = L_Z + 256, L_OS = L_F + 64,
              L_DUMP = L_OS + 256, L_END = L_DUMP + 256;
constexpr int F_PROG = L_F, F_BR = L_F + 4, F_T0 = L_F + 8, F_T1 = L_F + 12, F_QS = L_F + 16, F_MS = L_F + 32;

constexpr int NPRE = 2, I_K = 32, I_V = 64, I_T = 96, I_END = 120;
constexpr int E_Q = NPRE + 16, E_G = NPRE + I_K, E_K = NPRE + I_V, E_V = NPRE + I_T, E_END = NPRE + I_END;

constexpr int BURST = 20, LAG0 = 14, TAUG = 160, TAUK = 162, TAUV = 153, TAUT = 110, GAPK = 14, GAPV = 10;
constexpr int WEARLY = 28, IEARLY = 44, WDEPTH = 56;
constexpr int TIME_G = 70, TIME_A = 208, TIME_F = 360, TIME_E = 270;
constexpr int POLL_AT = 6, POLL_B = 7, POLL_C = 4;
constexpr int COFF = 64;
constexpr unsigned CODE_BYTES = 13824;

#define SB() do { asm volatile("" ::: "memory"); __builtin_amdgcn_sched_barrier(0); } while (0)

__device__ __forceinline__ float asf(unsigned u) { return __builtin_bit_cast(float, u); }
__device__ __forceinline__ unsigned asu(float f) { return __builtin_bit_cast(unsigned, f); }
__device__ __forceinline__ rsrc_t mkrs(const void* p, int bytes) { return __builtin_amdgcn_make_buffer_rsrc((void*)p, 0, bytes, 0x00020000); }
__device__ __forceinline__ float rcp(float x) { return __builtin_amdgcn_rcpf(x); }
__device__ __forceinline__ float ex2(float x) { return __builtin_amdgcn_exp2f(x); }
__device__ __forceinline__ void st16(rsrc_t r, int off, int soff, v4u v) { __builtin_amdgcn_raw_buffer_store_b128(v, r, off, soff, 16); }
__device__ __forceinline__ v4u ld16(rsrc_t r, int off, int soff = 0) { return __builtin_amdgcn_raw_buffer_load_b128(r, off, soff, 16); }
__device__ __forceinline__ unsigned pk(float lo, float hi) { f2 t = {lo, hi}; return __builtin_bit_cast(unsigned, __builtin_convertvector(t, bf16x2)); }
__device__ __forceinline__ float rbf(float f) { return (float)(__bf16)f; }
__device__ __forceinline__ float blo(unsigned u) { return asf(u << 16); }
__device__ __forceinline__ float bhi(unsigned u) { return asf(u & 0xffff0000u); }
template <class T> __device__ __forceinline__ LAS T* lp(unsigned off) { return (LAS T*)(size_t)off; }
__device__ __forceinline__ v2u trd(unsigned off) { return __builtin_bit_cast(v2u, __builtin_amdgcn_ds_read_tr16_b64_v4i16(lp<s4>(off))); }
__device__ __forceinline__ v4u cat(v2u a, v2u b) { return v4u{a.x, a.y, b.x, b.y}; }
template <int CTRL> __device__ __forceinline__ float dpp(float x) { return asf(__builtin_amdgcn_mov_dpp(asu(x), CTRL, 0xf, 0xf, true)); }
constexpr int ROR1 = 0x121, ROR2 = 0x122, ROR4 = 0x124, ROR8 = 0x128;
__device__ __forceinline__ float sum16x(float a, float b) {
    auto s = __builtin_amdgcn_permlane16_swap(asu(a), asu(b), false, false); return asf(s[0]) + asf(s[1]);
}
__device__ __forceinline__ float sum32x(float a, float b) {
    auto s = __builtin_amdgcn_permlane32_swap(asu(a), asu(b), false, false); return asf(s[0]) + asf(s[1]);
}
__device__ __forceinline__ float vmax(float a, float b) { float r; asm("v_max_f32 %0, %1, %2" : "=v"(r) : "v"(a), "v"(b)); return r; }
__device__ __forceinline__ float vmax3(float a, float b, float c) { float r; asm("v_max3_f32 %0, %1, %2, %3" : "=v"(r) : "v"(a), "v"(b), "v"(c)); return r; }
#define MAX4_ALL(N) \
    "v_max3_f32 %0, %2, %3, %4\n\tv_max_f32 %0, %0, %5\n\ts_nop 1\n\tv_max_f32_dpp %0, %0, %0 row_ror:" #N " row_mask:0xf bank_mask:0xf\n\t" \
    "v_mov_b32 %1, %0\n\ts_nop 1\n\tv_permlane16_swap_b32 %0, %1\n\tv_max_f32 %0, %0, %1\n\t" \
    "v_mov_b32 %1, %0\n\ts_nop 1\n\tv_permlane32_swap_b32 %0, %1\n\tv_max_f32 %0, %0, %1"
__device__ __forceinline__ float max4_ror4(float a, float b, float c, float d) { float r, t; asm(MAX4_ALL(4) : "=&v"(r), "=&v"(t) : "v"(a), "v"(b), "v"(c), "v"(d)); return r; }
__device__ __forceinline__ float max4_ror8(float a, float b, float c, float d) { float r, t; asm(MAX4_ALL(8) : "=&v"(r), "=&v"(t) : "v"(a), "v"(b), "v"(c), "v"(d)); return r; }
__device__ __forceinline__ float wave_sum(float x) {
    x += dpp<ROR8>(x); x += dpp<ROR4>(x); x += dpp<ROR2>(x); x += dpp<ROR1>(x);
    x = sum16x(x, x); return sum32x(x, x);
}
__device__ __forceinline__ f4 mfma32(v4u a, v4u b, f4 c) {
    return __builtin_amdgcn_mfma_f32_16x16x32_bf16(__builtin_bit_cast(bf16x8, a), __builtin_bit_cast(bf16x8, b), c, 0, 0, 0);
}
#define PIN4(a) asm volatile("" : "+v"((a)[0]), "+v"((a)[1]), "+v"((a)[2]), "+v"((a)[3]))
#define PIN8(a) asm volatile("" : "+v"((a)[0]), "+v"((a)[1]), "+v"((a)[2]), "+v"((a)[3]), "+v"((a)[4]), "+v"((a)[5]), "+v"((a)[6]), "+v"((a)[7]))
#define ZERO4 f4{0.f, 0.f, 0.f, 0.f}

template <int OFF> __device__ __forceinline__ void wait_word(unsigned fb, unsigned n) {
    unsigned t, c;
    SB();
    asm volatile("s_mov_b32 %1, 0x400000\n.Lw%=:\n\tds_read_b32 %0, %2 offset:%4\n\ts_waitcnt lgkmcnt(0)\n\tv_cmp_le_u32 vcc, %3, %0\n\ts_cbranch_vccnz .Le%=\n\t"
                 "s_sleep 1\n\ts_sub_u32 %1, %1, 1\n\ts_cbranch_scc0 .Lw%=\n.Le%=:"
                 : "=&v"(t), "=&s"(c) : "v"(fb), "s"(n), "i"(OFF - L_F) : "vcc", "scc", "memory");
    SB();
}
template <int OFF> __device__ __forceinline__ void wait_set(unsigned fl) {
    unsigned t, c;
    SB();
    asm volatile("s_mov_b32 %1, 0x400000\n.Lw%=:\n\tds_read_b32 %0, %2 offset:%3\n\ts_waitcnt lgkmcnt(0)\n\tv_cmp_eq_u32 vcc, 0, %0\n\ts_cbranch_vccz .Le%=\n\t"
                 "s_sub_u32 %1, %1, 1\n\ts_cbranch_scc0 .Lw%=\n.Le%=:"
                 : "=&v"(t), "=&s"(c) : "v"(fl), "i"(OFF - L_F) : "vcc", "scc", "memory");
    SB();
}
__device__ __forceinline__ unsigned now() { return (unsigned)__builtin_amdgcn_s_memrealtime(); }
template <int TICKS> __device__ __forceinline__ void wait_time(unsigned t0, unsigned fb, unsigned n) {
    unsigned t, c;
    SB();
    asm volatile(".Lw%=:\n\ts_memrealtime vcc\n\tds_read_b32 %0, %2\n\ts_waitcnt lgkmcnt(0)\n\ts_sub_u32 %1, vcc_lo, %4\n\ts_cmp_ge_i32 %1, %5\n\ts_cbranch_scc1 .Le%=\n\t"
                 "v_cmp_le_u32 vcc, %3, %0\n\ts_cbranch_vccz .Lw%=\n.Le%=:"
                 : "=&v"(t), "=&s"(c) : "v"(fb), "s"(n), "s"(t0), "i"(TICKS) : "vcc", "scc", "memory");
    SB();
}
template <int OFF> __device__ __forceinline__ void sync_read4(unsigned fl, unsigned da, v4u (&d)[4]) {
    unsigned t, c;
    asm volatile("s_mov_b32 %5, 0x400000\n.Lw%=:\n\tds_read_b32 %4, %6 offset:%8\n\tds_read_b128 %0, %7\n\tds_read_b128 %1, %7 offset:64\n\t"
                 "ds_read_b128 %2, %7 offset:128\n\tds_read_b128 %3, %7 offset:192\n\ts_waitcnt lgkmcnt(0)\n\t"
                 "v_cmp_eq_u32 vcc, 0, %4\n\ts_cbranch_vccz .Le%=\n\ts_sub_u32 %5, %5, 1\n\ts_cbranch_scc0 .Lw%=\n.Le%=:"
                 : "=&v"(d[0]), "=&v"(d[1]), "=&v"(d[2]), "=&v"(d[3]), "=&v"(t), "=&s"(c)
                 : "v"(fl), "v"(da), "i"(OFF - L_F) : "vcc", "scc", "memory");
    SB();
}
#define SL_READS "ds_read_b32 %4, %8\n\tds_read_b128 %0, %9\n\tds_read_b128 %1, %9 offset:64\n\tds_read_b128 %2, %9 offset:128\n\tds_read_b128 %3, %9 offset:192\n\t"
__device__ __forceinline__ float sync_lanes(unsigned fa, unsigned da, v4u (&d)[4], u64 object, float wsum) {
    unsigned t, c; float inv, u;
    SB();
    asm volatile(SL_READS "v_add_f32_dpp %6, %11, %11 row_ror:8 row_mask:0xf bank_mask:0xf\n\tv_mov_b32 %7, %6\n\ts_mov_b32 %5, 0x400000\n\ts_nop 0\n\t"
                 "v_permlane16_swap_b32 %6, %7\n\tv_add_f32 %6, %6, %7\n\tv_mov_b32 %7, %6\n\ts_nop 1\n\tv_permlane32_swap_b32 %6, %7\n\tv_add_f32 %6, %6, %7\n\tv_rcp_f32 %6, %6\n"
                 ".Lw%=:\n\ts_waitcnt lgkmcnt(4)\n\tv_cmp_ne_u32 vcc, 0x01010101, %4\n\ts_cbranch_vccz .Le%=\n\t" SL_READS
                 "s_cmp_lg_u64 %10, 0\n\ts_cbranch_scc1 .Le%=\n\ts_sub_u32 %5, %5, 1\n\ts_cbranch_scc0 .Lw%=\n.Le%=:\n\ts_waitcnt lgkmcnt(0)"
                 : "=&v"(d[0]), "=&v"(d[1]), "=&v"(d[2]), "=&v"(d[3]), "=&v"(t), "=&s"(c), "=&v"(inv), "=&v"(u)
                 : "v"(fa), "v"(da), "s"(object), "v"(wsum) : "vcc", "scc", "memory");
    SB();
    return inv;
}
#define POLL_CHK "s_nop 1\n\tv_cmp_ne_u64 vcc, %0, %5\n\ts_cbranch_vccz .Le%=\n\t"
template <int N> __device__ __forceinline__ void poll32(v2u& cnt, v2u want, int voff, v4u r, int soff) {
    unsigned c;
    SB();
    asm volatile("s_movk_i32 %1, 48\n.Lw%=:\n\ts_waitcnt vmcnt(20)\n\tbuffer_atomic_cmpswap %0, %2, %3, %4 offen sc0\n\t.rept %6\n\t" POLL_CHK ".endr\n\t"
                 "s_sub_u32 %1, %1, 1\n\ts_cbranch_scc0 .Lw%=\n.Le%=:"
                 : "+v"(cnt), "=&s"(c) : "v"(voff), "s"(r), "s"(soff), "v"(want), "i"(N) : "vcc", "scc", "memory");
    SB();
}
#define POLL_INIT(cnt, want, base, on, N) v2u cnt = {(on) ? (base) : 0u, (base) - 1u}, want = {(on) ? (base) + (N) : 0u, (base) - 1u}; asm volatile("" : "+v"(cnt), "+v"(want))
__device__ __forceinline__ void set_word(int off, unsigned v) { SB(); *(volatile LAS unsigned*)lp<unsigned>(off) = v; SB(); }

struct Buf { const void* p; unsigned bytes, flags; };
struct Params { Buf x, ws, wq, wg, wu, kc, wd, vc, wo, out; };
typedef const __attribute__((address_space(4))) v4u* kargs_t;
enum { A_X, A_WS, A_WQ, A_WG, A_WU, A_K, A_WD, A_V, A_WO, A_OUT };
__device__ __forceinline__ rsrc_t rs_of(v4u d, int bytes) { return mkrs((const void*)(((u64)d.y << 32) | d.x), bytes); }
#define KARGS() kargs_t ap = (kargs_t)__builtin_amdgcn_kernarg_segment_ptr(); asm volatile("" : "+s"(ap))

struct Ld { v4u rq, rg, ru, rk, rd, rv, ro; int vq, vg, vd, vo, vk[4]; int sq, sg, sd, so, kh, s4; unsigned fa; };
template <int LDS, int ADD> __device__ __forceinline__ void dma_tile(int voff, v4u r, int sbase) {
    int t;
    asm volatile("s_mov_b32 m0, %1\n\ts_add_u32 %0, %4, %2\n\tbuffer_load_dwordx4 %3, %5, %0 offen nt lds"
                 : "=&s"(t) : "i"(LDS), "i"(ADD), "v"(voff), "s"(sbase), "s"(r) : "m0", "scc", "memory");
}
template <int LDS> __device__ __forceinline__ void dma_block(const int (&voff)[4], v4u r, int soff) {
    asm volatile("s_mov_b32 m0, %0\n\ts_nop 0\n\tbuffer_load_dwordx4 %1, %5, %6 offen nt lds\n\tbuffer_load_dwordx4 %2, %5, %6 offen offset:1024 nt lds\n\t"
                 "buffer_load_dwordx4 %3, %5, %6 offen offset:2048 nt lds\n\tbuffer_load_dwordx4 %4, %5, %6 offen offset:3072 nt lds"
                 :: "i"(LDS), "v"(voff[0]), "v"(voff[1]), "v"(voff[2]), "v"(voff[3]), "s"(r), "s"(soff) : "m0", "memory");
}
constexpr int tile_row(int p) { return 16 * (p >> 1) + 4 * (p & 1); }
constexpr int due_at(int i) {
    int ns = 0;
    for (int k = BURST; k < i; k += 4) ns += k < I_K ? TAUG : k < I_V ? TAUK : k < I_T ? TAUV : TAUT;
    return LAG0 + ns / 10 + (i >= I_V ? GAPK : 0) + (i >= I_T ? GAPV : 0);
}
#define REPORT "s_getreg_b32 %1, hwreg(HW_REG_IB_STS)\n\ts_bfe_u32 %2, %1, 0x20016\n\ts_and_b32 %1, %1, 15\n\ts_lshl4_add_u32 %1, %2, %1\n\t" \
               "s_sub_u32 %2, %5, %1\n\tv_mov_b32 %0, %2\n\tds_write_b32 %4, %0\n\ts_waitcnt lgkmcnt(0)\n\t"
template <int I> __device__ __forceinline__ void ld_step(const Ld& c) {
    if constexpr (I >= BURST && (I & 3) == 0) {
        unsigned v, t, u, o;
        if constexpr (I < I_END)
            asm volatile(".Lw%=:\n\ts_memrealtime vcc\n\tds_read_b32 %3, %4 offset:12\n\t" REPORT "s_cmp_gt_u32 %1, %6\n\ts_cbranch_scc1 .Lw%=\n\t"
                         "v_readfirstlane_b32 %2, %3\n\ts_sub_u32 %2, vcc_lo, %2\n\ts_cmpk_lt_i32 %2, %7\n\ts_cbranch_scc1 .Lw%="
                         : "=&v"(v), "=&s"(t), "=&s"(u), "=&v"(o) : "v"(c.fa), "i"(NPRE + I), "i"((I < IEARLY ? WEARLY : WDEPTH) - 4), "i"(due_at(I)) : "vcc", "scc", "memory");
        else
            asm volatile("s_nop 1\n.Lw%=:\n\t" REPORT "s_cmp_lg_u32 %1, 0\n\ts_cbranch_scc1 .Lw%="
                         : "=&v"(v), "=&s"(t), "=&s"(u), "=&v"(o) : "v"(c.fa), "i"(NPRE + I) : "scc", "memory");
    }
    if constexpr (I < 16) dma_tile<L_WQ + I * 1024, tile_row(I) * 8192>(c.vq, c.rq, c.sq);
    else if constexpr (I < I_K) dma_tile<L_G + (I - 16) * 1024, tile_row(I & 7) * FF * 2>(c.vg, I < 24 ? c.rg : c.ru, c.sg);
    else if constexpr (I < I_T) {
        constexpr int j = (I & 31) >> 2;
        if constexpr ((I & 3) == 0) dma_block<(I < I_V ? L_K : L_V) + j * 4096>(c.vk, I < I_V ? c.rk : c.rv, c.kh + j * 131072 + ((c.s4 + j * 4096) & 0x1f000));
    } else if constexpr (I < I_END) {
        constexpr int q = (I - I_T) / 3, r = (I - I_T) % 3;
        if constexpr (r == 0) dma_tile<L_D + q * 1024, tile_row(q) * 1024>(c.vd, c.rd, c.sd);
        else dma_tile<L_WO + (2 * q + r - 1) * 1024, tile_row(2 * q + r - 1) * 1024>(c.vo, c.ro, c.so);
    }
}
template <int I0, int... Is> __device__ __forceinline__ void ld_all(const Ld& c, std::integer_sequence<int, Is...>) { (ld_step<I0 + Is>(c), ...); }

extern "C" __global__ void __launch_bounds__(448, 2) fused_decode(Params) {
    u64 pc;
    unsigned k0, k1, k2;
    asm volatile("v_readfirstlane_b32 %0, %5\n\ts_getpc_b64 %3\n\ts_cmp_lt_u32 %0, 64\n\ts_cbranch_scc0 .Ls%=\n\t"
                 "s_load_dword %0, %4, 0x0\n\ts_load_dword %1, %4, 0x40\n\ts_load_dword %2, %4, 0x80\n.Ls%=:"
                 : "=&s"(k0), "=&s"(k1), "=&s"(k2), "=&s"(pc) : "s"(__builtin_amdgcn_kernarg_segment_ptr()), "v"(threadIdx.x) : "scc");
    SB();
    __shared__ __attribute__((aligned(16))) unsigned char lds[L_END];
    asm volatile("" :: "v"((LAS unsigned char*)lds));
    const int tid = threadIdx.x, lane = tid & 63, wave = __builtin_amdgcn_readfirstlane(tid >> 6);
    const int b = blockIdx.x, x = b & 7, s = b >> 3;
    const int rho = lane >> 3, c8 = lane & 7, a = lane >> 4;
    const int head = 4 * x + (s >> 3), cb = s & 7;
    const int yb = (cb + 2) & 7;
    unsigned fb = L_F;
    asm volatile("" : "+v"(fb));
    int wv1 = wave, wv2 = wave, wv3 = wave;
    asm volatile("" : "+s"(wv1), "+s"(wv2), "+s"(wv3));
    if (wv3 != 0) {
        const v4u rc = {(unsigned)pc & ~255u, (unsigned)(pc >> 32) & 0xffffu, CODE_BYTES, 0x00020000u};
        asm volatile("s_mov_b32 m0, %0\n\ts_nop 0\n\tbuffer_load_dword %1, %2, 0 offen lds" :: "i"(L_DUMP), "v"((tid - 64) * 128), "s"(rc) : "m0", "memory");
        *lp<unsigned>(L_Z + lane * 4) = 0u;
        *lp<unsigned>(L_F + (lane & 15) * 4) = 0u;
        *lp<unsigned>(L_OS + lane * 4) = 0u;
        asm volatile("s_waitcnt lgkmcnt(0)\n\ts_barrier" ::: "memory");
        SB();
    }
    if (wave >= 3) {
        const int w = wave - 3, n = lane & 15, h = lane & 3;
        const unsigned fl = L_F + h * 4;
        KARGS();
        const rsrc_t ws = rs_of(ap[A_WS], WS_END);
        const int tr = (a & 1) * 4 + ((lane >> 2) & 3);
        unsigned ta = (a >> 1) * 2048 + tr * 128 + ((w ^ (tr >> 1)) * 32) + h * 8;
        int goff = n < 2 ? (a * 2 + n) * 16 : OOB;
        int qso = WS_Q + (x * 4 + (s >> 3)) * 4096 + cb * 512 + w * 128, fso = WS_F + (head * 16 + cb) * 512 + w * 128, gqo = WS_Q + x * 16384 + w * 1024;
        unsigned xqa = L_XN + (s >> 3) * 256 + a * 16, xga = L_XN + cb * 128 + a * 16, woa = L_WO + ta;
        asm volatile("" : "+v"(ta), "+v"(goff), "+v"(xqa), "+v"(xga), "+v"(woa));
        asm volatile("" : "+s"(qso), "+s"(fso), "+s"(gqo));
        wait_word<F_PROG>(fb, 1);
        const unsigned tx = __builtin_amdgcn_readfirstlane(*lp<unsigned>(F_T0));
        const v4u xa = *lp<v4u>(L_X + lane * 16);
        {
            float v[8];
#pragma unroll
            for (int i = 0; i < 4; ++i) { v[2 * i] = blo(xa[i]); v[2 * i + 1] = bhi(xa[i]); }
            const float mean = wave_sum(((v[0] + v[1]) + (v[2] + v[3])) + ((v[4] + v[5]) + (v[6] + v[7]))) * (1.f / 512.f);
            float q = 0.f;
#pragma unroll
            for (int i = 0; i < 8; ++i) { const float d = v[i] - mean; q = fmaf(d, d, q); }
            const float var = wave_sum(q) * (1.f / 512.f);
            const float mb = rbf(mean), rs = rcp(rbf(__builtin_amdgcn_sqrtf(rbf(rbf(var) + 1e-5f))));
            v4u xn;
#pragma unroll
            for (int i = 0; i < 4; ++i) { const unsigned u = pk(v[2 * i] - mb, v[2 * i + 1] - mb); xn[i] = pk(blo(u) * rs, bhi(u) * rs); }
            *lp<v4u>(L_XN + lane * 16) = xn;
        }
        SB();
        v4u xq[4], xg[2];
#pragma unroll
        for (int k = 0; k < 4; ++k) xq[k] = *lp<v4u>(xqa + k * 64);
#pragma unroll
        for (int k = 0; k < 2; ++k) xg[k] = *lp<v4u>(xga + k * 64);
        PIN4(xq);
        int coff = (w == 3 && lane < 8) ? lane * 512 : OOB, cao = WS_C + x * 4096;
        asm volatile("" : "+v"(xg[0]), "+v"(xg[1]), "+v"(coff));
        asm volatile("" : "+s"(cao));
        wait_word<F_PROG>(fb, E_Q);
        const unsigned thi = *lp<unsigned>(L_EP) + 1u;
        {
            f4 d = ZERO4;
#pragma unroll
            for (int k = 0; k < 4; ++k) d = mfma32(cat(trd(ta + L_WQ + k * 4096), trd(ta + L_WQ + k * 4096 + 1024)), xq[k], d);
            st16(ws, goff, qso, v4u{asu(n ? d[2] : d[0]), thi, asu(n ? d[3] : d[1]), thi});
        }
        auto gate_up = [&]() __attribute__((always_inline)) {
            wait_word<F_PROG>(fb, E_G);
#pragma unroll
            for (int kind = 0; kind < 2; ++kind) {
                f4 d = ZERO4;
#pragma unroll
                for (int k = 0; k < 2; ++k) d = mfma32(cat(trd(ta + L_G + kind * 8192 + k * 4096), trd(ta + L_G + kind * 8192 + k * 4096 + 1024)), xg[k], d);
                st16(ws, head < 28 ? goff : OOB, fso + kind * 4096, v4u{asu(n ? d[2] : d[0]), thi, asu(n ? d[3] : d[1]), thi});
            }
        };
        unsigned ka[4], va[4], pra[2];
#pragma unroll
        for (int k = 0; k < 4; ++k) ka[k] = L_K + (w * 32 + n) * 256 + (((4 * k + a) ^ (h * 4 + (n >> 2))) * 16);
#pragma unroll
        for (int i = 0; i < 4; ++i) {
            const int q4 = (lane >> 2) & 3, u = i & 1;
            va[i] = L_V + (8 * a + 4 * u + q4) * 256 + (((4 * w + (i & 2) + (h >> 1)) ^ (q4 * 4 + ((2 * a + u) & 3))) * 16) + (h & 1) * 8;
        }
#pragma unroll
        for (int t = 0; t < 2; ++t) pra[t] = (n >> 2) == t ? L_P + h * 272 + a * 16 : L_Z;
        unsigned qa = L_Q + h * 272 + a * 16, qwa = L_Q + w * 272 + lane * 4, pwa = L_P + h * 272 + w * 64 + ((n >> 2) & 1) * 32 + a * 8, mla = L_ML + h * 16, mwa = mla + w * 4;
        int ooff = n < 8 ? h * 16384 + (n >> 2) * 64 + a * 16 : OOB, gboff = rho * 2048 + c8 * 16;
        unsigned owa = L_O + (w * 32 + c8 * 4 + a) * 2, ora = L_O + a * 16, fra = L_OS + lane * 4, fwa = fra + w;
        int oso = WS_O + (4 * x * 32 + s) * 512 + w * 128, gso = WS_O + head * 16384 + w * 128, sso = WS_S + (cb * 64 + head) * 512 + w * 128;
        int cco = WS_C + cb * 4096 + (x & 3) * 128 + COFF;
        PIN4(ka); PIN4(va);
        asm volatile("" : "+v"(pra[0]), "+v"(pra[1]), "+v"(qa), "+v"(qwa), "+v"(pwa), "+v"(mla), "+v"(mwa), "+v"(ooff), "+v"(gboff), "+v"(owa), "+v"(ora), "+v"(fra), "+v"(fwa));
        int gs[4] = {gso, gso + 512, gso + 1024, gso + 1536};
        asm volatile("" : "+s"(oso), "+s"(gs[0]), "+s"(gs[1]), "+s"(gs[2]), "+s"(gs[3]), "+s"(sso), "+s"(cco));
        v4u g[4];
#define LDQ() _Pragma("unroll") for (int r = 0; r < 4; ++r) g[r] = ld16(ws, lane * 16, gqo + r * 4096)
        wait_time<TIME_A>(tx, fb, E_G);
        if ((unsigned)__builtin_amdgcn_readfirstlane(*(volatile LAS unsigned*)lp<unsigned>(F_PROG)) >= E_G) {
            gate_up();
            wait_time<TIME_A>(tx, fb, E_V - 8);
            LDQ();
        } else {
            LDQ();
            gate_up();
        }
        wait_word<F_PROG>(fb, E_K);
        v4u K[8];
#pragma unroll
        for (int i = 0; i < 8; ++i) K[i] = *lp<v4u>(ka[i & 3] + (i >> 2) * 4096);
        PIN8(K);
        SB();
        {
            auto qbad = [&]() __attribute__((always_inline)) {
                const unsigned bad = (g[0].y ^ thi) | (g[0].w ^ thi) | (g[1].y ^ thi) | (g[1].w ^ thi) | (g[2].y ^ thi) | (g[2].w ^ thi) | (g[3].y ^ thi) | (g[3].w ^ thi);
                return __any(bad != 0);
            };
            int spinA = 0;
            if (__builtin_expect(qbad(), 0)) {
                do { asm volatile("" ::: "memory"); LDQ(); ++spinA; } while (qbad() && spinA < SPIN_CAP);
            }
            *lp<unsigned>(qwa) = pk((asf(g[0].x) + asf(g[1].x)) + (asf(g[2].x) + asf(g[3].x)), (asf(g[0].z) + asf(g[1].z)) + (asf(g[2].z) + asf(g[3].z)));
        }
        set_word(F_QS + w * 4, 1);
        v4u VA[6];
#define RDV() _Pragma("unroll") for (int i = 0; i < 6; ++i) VA[i] = cat(trd(va[(i & 1) * 2] + (i >> 1) * 8192), trd(va[(i & 1) * 2 + 1] + (i >> 1) * 8192))
        v4u qf[4];
        sync_read4<F_QS>(fl, qa, qf);
        const unsigned vprog = *(volatile LAS unsigned*)lp<unsigned>(F_PROG);
        asm volatile("" ::: "memory");
        RDV();
        SB();
        {
            f4 d0 = ZERO4, d1 = ZERO4;
#pragma unroll
            for (int k = 0; k < 4; ++k) { d0 = mfma32(K[k], qf[k], d0); d1 = mfma32(K[4 + k], qf[k], d1); }
            const f4 d = (n & 4) ? d1 : d0;
            float sc[4];
#pragma unroll
            for (int i = 0; i < 4; i += 2) {
                const unsigned u = pk(d[i], d[i + 1]);
                const unsigned v = pk(blo(u) * 0.08838834764831845f, bhi(u) * 0.08838834764831845f);
                sc[i] = blo(v); sc[i + 1] = bhi(v);
            }
            const float mx = max4_ror4(sc[0], sc[1], sc[2], sc[3]), m = mx * LOG2E;
#pragma unroll
            for (int i = 0; i < 4; ++i) sc[i] = ex2((sc[i] - mx) * LOG2E);
            *lp<v2u>(pwa) = v2u{pk(sc[0], sc[1]), pk(sc[2], sc[3])};
            float l = (sc[0] + sc[1]) + (sc[2] + sc[3]);
            l += dpp<ROR4>(l);
            l = sum16x(l, l); l = sum32x(l, l);
            *lp<float>(mwa) = m; *lp<float>(mwa + 64) = l;
        }
        set_word(F_MS + w * 4, 1);
        f4 m4, l4;
        {
            unsigned t, c;
            asm volatile("s_mov_b32 %3, 0x400000\n.Lw%=:\n\tds_read_b32 %2, %4 offset:%6\n\tds_read_b128 %0, %5\n\tds_read_b128 %1, %5 offset:64\n\ts_waitcnt lgkmcnt(0)\n\t"
                         "v_cmp_eq_u32 vcc, 0, %2\n\ts_cbranch_vccz .Le%=\n\ts_sub_u32 %3, %3, 1\n\ts_cbranch_scc0 .Lw%=\n.Le%=:"
                         : "=&v"(m4), "=&v"(l4), "=&v"(t), "=&s"(c) : "v"(fl), "v"(mla), "i"(F_MS - L_F) : "vcc", "scc", "memory");
            SB();
        }
        v4u P[8];
#pragma unroll
        for (int i = 0; i < 8; ++i) P[i] = *lp<v4u>(pra[i & 1] + (i >> 1) * 64);
        SB();
        float sf[4];
        unsigned lse;
        {
            const float M = vmax(vmax3(m4[0], m4[1], m4[2]), m4[3]);
#pragma unroll
            for (int k = 0; k < 4; ++k) sf[k] = ex2(m4[k] - M);
            const float L = (l4[0] * sf[0] + l4[1] * sf[1]) + (l4[2] * sf[2] + l4[3] * sf[3]);
            const float r = rcp(L);
#pragma unroll
            for (int k = 0; k < 4; ++k) sf[k] *= r;
            lse = asu(M + __builtin_amdgcn_logf(L));
        }
        PIN4(sf);
        unsigned lt0 = (lse & 0xffff0000u) | (thi & 0xffffu), lt1 = (lse << 16) | (thi & 0xffffu);
        float ref = asf(__builtin_amdgcn_readlane(lse, s >> 3));
        asm volatile("" : "+v"(lt0), "+v"(lt1));
        asm volatile("" : "+s"(ref));
#define PV(k) mfma32(VA[2 * (k) + 1], P[2 * (k) + 1], mfma32(VA[2 * (k)], P[2 * (k)], ZERO4))
        if ((unsigned)__builtin_amdgcn_readfirstlane(vprog) < E_V - 8) { wait_word<F_PROG>(fb, E_V - 8); RDV(); }
        f4 o = PV(0) * sf[0];
        o += PV(1) * sf[1];
        o += PV(2) * sf[2];
        asm volatile("" : "+v"(o));
        SB();
        {
            v2u v0, v1, v2, v3;
            unsigned t, c;
            asm volatile("s_mov_b32 %5, 0x400000\n.Lw%=:\n\tds_read_b32 %4, %10\n\tds_read_b64_tr_b16 %0, %6 offset:24576\n\tds_read_b64_tr_b16 %1, %7 offset:24576\n\t"
                         "ds_read_b64_tr_b16 %2, %8 offset:24576\n\tds_read_b64_tr_b16 %3, %9 offset:24576\n\ts_waitcnt lgkmcnt(0)\n\t"
                         "v_cmp_le_u32 vcc, %11, %4\n\ts_cbranch_vccnz .Le%=\n\ts_sub_u32 %5, %5, 1\n\ts_cbranch_scc0 .Lw%=\n.Le%=:"
                         : "=&v"(v0), "=&v"(v1), "=&v"(v2), "=&v"(v3), "=&v"(t), "=&s"(c)
                         : "v"(va[0]), "v"(va[1]), "v"(va[2]), "v"(va[3]), "v"(fb), "i"(E_V) : "vcc", "scc", "memory");
            SB();
            (void)__builtin_amdgcn_raw_ptr_buffer_atomic_add_i32(1, ws, coff, cao, 0);
            o += mfma32(cat(v2, v3), P[7], mfma32(cat(v0, v1), P[6], ZERO4)) * sf[3];
            st16(ws, ooff, oso, v4u{pk(o[0], o[1]), lt0, pk(o[2], o[3]), lt1});
        }
        wait_set<F_BR>(fb);
        v4u ov[4];
#define LDO() _Pragma("unroll") for (int i = 0; i < 4; ++i) { ov[i] = ld16(ws, gboff, gs[i]); SB(); }
        LDO();
        wait_word<F_PROG>(fb, E_END);
        v4u W[4];
#pragma unroll
        for (int k = 0; k < 4; ++k) W[k] = cat(trd(woa + k * 4096), trd(woa + k * 4096 + 1024));
        PIN4(W);
        SB();
        float wsum;
        auto merge = [&](float r) __attribute__((always_inline)) {
            f4 acc; unsigned tg;
#pragma unroll
            for (int i = 0; i < 4; ++i) {
                const float wt = ex2(asf(__builtin_amdgcn_perm(ov[i].y, ov[i].w, 0x07060302u)) - r);
                const f4 v = f4{blo(ov[i].x), bhi(ov[i].x), blo(ov[i].z), bhi(ov[i].z)};
                if (i == 0) { acc = v * wt; wsum = wt; } else { acc += v * wt; wsum += wt; }
                if (i < 3) {
                    unsigned t = (ov[i].y ^ thi) | (ov[i].w ^ thi);
                    asm volatile("" : "+v"(t));
                    tg = i ? tg | t : t;
                    asm volatile("" : "+v"(acc), "+v"(wsum), "+v"(tg));
                    SB();
                }
            }
            float a0 = acc[0], a1 = acc[1], a2 = acc[2], a3 = acc[3];
            unsigned t, u; u64 bad;
            asm volatile("v_bitop3_b32 %4, %7, %8, %9 bitop3:0x7e\n\tv_max_u32 %5, 0x53800000, %11\n\t"
                         "v_permlane32_swap_b32 %0, %2\n\tv_permlane32_swap_b32 %1, %3\n\tv_add_f32 %0, %0, %2\n\tv_add_f32 %1, %1, %3\n\t"
                         "v_bitop3_b32 %4, %4, %10, %14 bitop3:0xa8\n\tv_xor_b32 %5, 0x53800000, %5\n\t"
                         "v_permlane16_swap_b32 %0, %1\n\tv_add_f32 %0, %0, %1\n\tv_or_b32 %4, %4, %5\n\tv_cmp_ne_u32 vcc, 0, %4\n\t"
                         "v_add_f32_dpp %0, %0, %0 row_ror:8 row_mask:0xf bank_mask:0xf\n\tv_cvt_pk_bf16_f32 %0, %0, %0\n\tv_cndmask_b32 %4, 1, 0, vcc\n\t"
                         "ds_write_b16 %12, %0\n\tds_write_b8 %13, %4\n\ts_mov_b64 %6, vcc"
                         : "+v"(a0), "+v"(a1), "+v"(a2), "+v"(a3), "=&v"(t), "=&v"(u), "=s"(bad)
                         : "v"(ov[3].y), "v"(ov[3].w), "v"(thi), "v"(tg), "v"(wsum), "v"(owa), "v"(fwa), "s"(0xffffu) : "vcc", "memory");
            return bad;
        };
        const u64 object = merge(ref);
        (void)__builtin_amdgcn_raw_ptr_buffer_atomic_add_i32(1, ws, coff, cco, 0);
        v4u of[4];
        float inv = sync_lanes(fra, ora, of, object, wsum);
        if (__builtin_expect(object != 0, 0)) {
            int spinB = 0;
            while (__any((((ov[0].y ^ thi) | (ov[0].w ^ thi) | (ov[1].y ^ thi) | (ov[1].w ^ thi) | (ov[2].y ^ thi) | (ov[2].w ^ thi) | (ov[3].y ^ thi) | (ov[3].w ^ thi)) & 0xffffu) != 0) &&
                   spinB < SPIN_CAP) { LDO(); ++spinB; }
            const float M = max4_ror8(asf(__builtin_amdgcn_perm(ov[0].y, ov[0].w, 0x07060302u)), asf(__builtin_amdgcn_perm(ov[1].y, ov[1].w, 0x07060302u)),
                                      asf(__builtin_amdgcn_perm(ov[2].y, ov[2].w, 0x07060302u)), asf(__builtin_amdgcn_perm(ov[3].y, ov[3].w, 0x07060302u)));
            float r = ref;
#pragma nounroll
            for (int pass = 0; pass < 2 && merge(r) != 0; ++pass) r = M;
            *(volatile LAS unsigned char*)lp<unsigned char>(fwa) = 1;
            inv = sync_lanes(fra, ora, of, 0, wsum);
        }
        {
            const f4 d = mfma32(W[3], of[3], mfma32(W[2], of[2], mfma32(W[1], of[1], mfma32(W[0], of[0], ZERO4))));
            st16(ws, goff, sso, v4u{asu((n ? d[2] : d[0]) * inv), thi, asu((n ? d[3] : d[1]) * inv), thi});
        }
        return;
    }

    if (wv1 == 2) {
        KARGS();
        const v4u wsd = ap[A_WS];
        const rsrc_t ws = rs_of(wsd, WS_END);
        const int col = x * 64 + s * 2;
        wait_word<F_T0>(fb, 1);
        const unsigned tx = __builtin_amdgcn_readfirstlane(*lp<unsigned>(F_T0));
        st16(ws, (s == 0 && lane < 8) ? lane * 16 : OOB, WS_T + x * 128, v4u{tx, tx, tx, tx});
        {
            wait_time<TIME_G>(tx, fb, 200);
            const unsigned tg = __builtin_amdgcn_raw_buffer_load_b32(ws, c8 * 128, WS_T, 16);
            unsigned lead = tx - tg;
            lead = lead <= 100u ? lead : 0u;
            lead = max(lead, (unsigned)__builtin_amdgcn_mov_dpp(lead, 0xB1, 0xf, 0xf, true));
            lead = max(lead, (unsigned)__builtin_amdgcn_mov_dpp(lead, 0x4E, 0xf, 0xf, true));
            lead = max(lead, (unsigned)__builtin_amdgcn_mov_dpp(lead, ROR4, 0xf, 0xf, true));
            set_word(F_T1, tx - lead);
        }
        wait_word<F_PROG>(fb, E_Q);
        const unsigned thi = *lp<unsigned>(L_EP) + 1u;
        const unsigned xv = *lp<u16>(L_X + ((col + (lane & 3) * 2 + (a & 1)) & 511) * 2);
        int so = WS_S + x * 32768 + s * 16, so1 = so + 8192, so2 = so + 16384, so3 = so + 24576;
        int bpo = WS_C + (x * 32 + (s & ~3)) * 128, cpo = WS_C + (x * 32 + s) * 128 + COFF;
        int roff = (lane >> 2) * 512 + (lane & 3) * 16, rlast = lane < 48 ? roff : OOB, poff = lane == 0 ? 0 : OOB, qoff = lane < 4 ? lane * 128 : OOB;
        unsigned wtag = lane < 48 ? thi : 0u;
        const unsigned baseB = *lp<unsigned>(L_EP + 8), baseC = *lp<unsigned>(L_EP + 16 + (lane & 3) * 4);
        POLL_INIT(cntB, wantB, baseB, lane == 0, 32u);
        POLL_INIT(cnt, want, baseC, lane < 4, 8u);
        int ooff = (lane & 0x2c) == 0 ? (lane & 3) * 4 + a * 2 : OOB;
        float xf = blo(xv);
        asm volatile("" : "+v"(roff), "+v"(rlast), "+v"(poff), "+v"(qoff), "+v"(wtag), "+v"(ooff), "+v"(xf));
        asm volatile("" : "+s"(so), "+s"(so1), "+s"(so2), "+s"(so3), "+s"(bpo), "+s"(cpo));
        const v4u od = ap[A_OUT];
        int oso = col * 2;
        asm volatile("" : "+s"(oso) : "s"(od));
        wait_word<F_PROG>(fb, E_V + POLL_AT);
        poll32<POLL_B>(cntB, wantB, poff, wsd, bpo);
        set_word(F_BR, 1);
        if (s & 3) { asm volatile("s_waitcnt vmcnt(0)" :: "v"(cntB) : "memory"); return; }
        wait_word<L_OS>(fb, 1);
        poll32<POLL_C>(cnt, want, qoff, wsd, cpo);
        unsigned res;
        int spinC = 0;
        for (;; ++spinC) {
            asm volatile("" ::: "memory");
            const v4u y0 = ld16(ws, roff, so2), y1 = ld16(ws, rlast, so3), s0 = ld16(ws, roff, so), s1 = ld16(ws, roff, so1);
            unsigned t0 = (y0.y ^ thi) | (y0.w ^ thi), t1 = (y1.y ^ wtag) | (y1.w ^ wtag), t2 = (s0.y ^ thi) | (s0.w ^ thi), t3 = (s1.y ^ thi) | (s1.w ^ thi);
            asm("" : "+v"(t0)); asm("" : "+v"(t1)); asm("" : "+v"(t2)); asm("" : "+v"(t3));
            u64 miss = __builtin_amdgcn_uicmp(t0 | t1 | t2 | t3, 0u, 33  );
            asm volatile("" : "+s"(miss));
            float e = sum16x(sum32x(asf(s0.x) + asf(s1.x), asf(y0.x) + asf(y1.x)), sum32x(asf(s0.z) + asf(s1.z), asf(y0.z) + asf(y1.z)));
            e += dpp<ROR8>(e); e += dpp<ROR4>(e);
            e = rbf(e);
            const auto sy = __builtin_amdgcn_permlane32_swap(asu(e), asu(e), false, false);
            const float yv = asf(sy[1]);
            const float gv = rbf(yv * rcp(1.f + ex2(-LOG2E * yv)));
            res = pk(xf + rbf(asf(sy[0]) * gv), 0.f);
            asm volatile("" : "+v"(res));
            if (__builtin_expect(miss == 0, 1) || spinC >= SPIN_CAP) break;
        }
        asm volatile("buffer_store_short %0, %1, %2, %3 offen" :: "v"(res), "v"(ooff), "s"(od), "s"(oso) : "memory");
        asm volatile("" :: "v"(cnt), "v"(cntB));
        return;
    }
    if (wv2 == 1) {
        if (head >= 28 && b != 255) return;
        KARGS();
        const rsrc_t ws = rs_of(ap[A_WS], WS_END);
        const int n = lane & 15;
        wait_word<F_PROG>(fb, E_Q);
        if (b == 255) {
            const unsigned tg = *lp<unsigned>(L_EP) + 1u;
            wait_time<TIME_E>(__builtin_amdgcn_readfirstlane(*lp<unsigned>(F_T0)), fb, E_V + 8);
            for (int spin = 0; spin < SPIN_CAP; ++spin) {
                asm volatile("" ::: "memory");
                unsigned d = 0;
#pragma unroll
                for (int k = 0; k < 4; ++k) d |= __builtin_amdgcn_raw_buffer_load_b32(ws, lane * 512, WS_Q + 4 + k * 32768, 16) ^ tg;
                if (!__any(d != 0)) break;
                __builtin_amdgcn_s_sleep(32);
            }
            wait_word<L_OS>(fb, 0x01010101u);
            st16(ws, lane < 8 ? lane * 16 : OOB, WS_EPOCH, v4u{tg, tg, tg, tg});
            return;
        }
        wait_time<TIME_F>(__builtin_amdgcn_readfirstlane(*lp<unsigned>(F_T0)), fb, E_V + 8);
        const unsigned thi = *lp<unsigned>(L_EP) + 1u;
        v4u fv[8];
#define LDF() _Pragma("unroll") for (int i = 0; i < 8; ++i) fv[i] = ld16(ws, lane * 16 + (i & 3) * 1024, WS_F + head * 8192 + (i >> 2) * 4096)
        LDF();
        auto fbad = [&]() __attribute__((always_inline)) {
            unsigned bh = 0;
#pragma unroll
            for (int i = 0; i < 8; ++i) bh |= (fv[i].y ^ thi) | (fv[i].w ^ thi);
            return __any(bh != 0);
        };
        int spinF = 0;
        if (__builtin_expect(fbad(), 0)) {
            do { asm volatile("" ::: "memory"); LDF(); ++spinF; } while (fbad() && spinF < SPIN_CAP);
        }
        float gt = sum32x((asf(fv[0].x) + asf(fv[1].x)) + (asf(fv[2].x) + asf(fv[3].x)), (asf(fv[0].z) + asf(fv[1].z)) + (asf(fv[2].z) + asf(fv[3].z)));
        float ut = sum32x((asf(fv[4].x) + asf(fv[5].x)) + (asf(fv[6].x) + asf(fv[7].x)), (asf(fv[4].z) + asf(fv[5].z)) + (asf(fv[6].z) + asf(fv[7].z)));
        const unsigned gu = pk(gt, ut);
        gt = blo(gu); ut = bhi(gu);
        const float z = gt * fmaf(gt * gt, -2.f * LOG2E * 0.7978845608028654f * 0.044715f, -2.f * LOG2E * 0.7978845608028654f);
        const float ge = rbf(gt * rcp(1.f + ex2(z)));
        *lp<u16>(L_H + (lane & 31) * 4 + (lane >> 5) * 2) = (u16)pk(ge * ut, 0.f);
        SB();
        v4u H[8];
#pragma unroll
        for (int i = 0; i < 8; ++i) H[i] = *lp<v4u>(((n >> 1) == (i >> 1) ? L_H + a * 16 : L_Z) + (i & 1) * 64);
        unsigned da[4];
        {
            const int tr = (a & 1) * 4 + ((lane >> 2) & 3);
#pragma unroll
            for (int t = 0; t < 4; ++t) da[t] = L_D + (a >> 1) * 2048 + tr * 128 + ((t ^ (tr >> 1)) * 32) + (lane & 3) * 8;
        }
        int yoff = n < 8 ? (n >> 1) * 128 + (a * 2 + (n & 1)) * 16 : OOB, yso = WS_S + (yb * 64 + 32 + head) * 512;
        PIN8(H); PIN4(da);
        asm volatile("" : "+v"(yoff));
        asm volatile("" : "+s"(yso));
        wait_word<F_PROG>(fb, E_END);
        f4 d = ZERO4, d2 = ZERO4;
#pragma unroll
        for (int t = 0; t < 4; ++t) {
            d = mfma32(cat(trd(da[t]), trd(da[t] + 1024)), H[2 * t], d);
            d2 = mfma32(cat(trd(da[t] + 4096), trd(da[t] + 5120)), H[2 * t + 1], d2);
        }
        d += d2;
        st16(ws, yoff, yso, v4u{asu((n & 1) ? d[2] : d[0]), thi, asu((n & 1) ? d[3] : d[1]), thi});
        return;
    }
    {
        Ld c;
        KARGS();
        const v4u rx = ap[A_X], rw = ap[A_WS];
        c.rq = ap[A_WQ]; c.rg = ap[A_WG]; c.ru = ap[A_WU]; c.rk = ap[A_K]; c.rd = ap[A_WD]; c.rv = ap[A_V]; c.ro = ap[A_WO];
        SB();
        const int trow = (rho >> 2) * 8 + (rho & 3), tcol = (c8 ^ (rho & 6)) * 16;
        c.vq = trow * 8192 + tcol;
        c.sq = (s >> 3) * 128 * 8192 + x * 1024 + cb * 128;
        int xo = lane * 16;
        asm volatile("" : "+v"(c.vq), "+v"(xo));
        asm volatile("" : "+s"(c.sq));
        asm volatile("s_barrier" ::: "memory");
        SB();
        asm volatile("" :: "s"(k0), "s"(k1), "s"(k2), "s"(rx), "s"(rw), "s"(c.rq), "s"(c.rg), "s"(c.ru), "s"(c.rk), "s"(c.rd), "s"(c.rv), "s"(c.ro));
        asm volatile("s_mov_b32 m0, %0\n\ts_nop 0\n\tbuffer_load_dwordx4 %1, %2, 0 offen lds" :: "i"(L_X), "v"(xo), "s"(rx) : "m0", "memory");
        ld_all<0>(c, std::make_integer_sequence<int, 16>{});
        c.vg = trow * FF * 2 + tcol; c.vd = trow * 1024 + tcol;
        c.vo = c.vd;
#pragma unroll
        for (int j = 0; j < 4; ++j) c.vk[j] = a * 256 + (((lane & 15) ^ (a * 4 + j)) * 16);
        c.sg = cb * 64 * FF * 2 + head * 128; c.sd = head * 65536 + yb * 128; c.so = head * 128 * 1024 + cb * 128;
        c.kh = (x * SK + (SK - WIN)) * 256; c.s4 = s * 4096; c.fa = fb;
        if (head >= 28) { c.vg = OOB; c.vd = OOB; }
        int e0 = WS_EPOCH, e1 = WS_C + (x * 32 + (s & ~3)) * 128, e2 = WS_C + (x * 32 + s + lane - 4) * 128 + COFF;
        asm("" : "+v"(e0), "+v"(e1), "+v"(e2));
        int eo = lane < 2 ? e0 : e1;
        eo = lane < 3 ? eo : OOB;
        eo = ((lane >> 2) == 1 && (s & 3) == 0) ? e2 : eo;
        asm volatile("s_mov_b32 m0, %0\n\ts_nop 0\n\tbuffer_load_dword %1, %2, 0 offen sc1 lds" :: "i"(L_EP), "v"(eo), "s"(rw) : "m0", "memory");
        ld_all<16>(c, std::make_integer_sequence<int, BURST - 16>{});
        { unsigned t; asm volatile("v_mov_b32 %0, %1\n\tds_write_b32 %2, %0 offset:12\n\tds_write_b32 %2, %0 offset:8" : "=&v"(t) : "s"(now() | 1u), "v"(fb) : "memory"); }
        ld_all<BURST>(c, std::make_integer_sequence<int, I_END + 1 - BURST>{});
    }
}

extern "C" void kernel_launch(void* const* d_in, const int* in_sizes, int n_in,
                              void* d_out, int out_size, void* d_ws, size_t ws_size,
                              hipStream_t stream) {
    const unsigned FL = 0x00020000u;
    Params p;
    p.x = Buf{d_in[0], 1024u, FL};
    p.ws = Buf{d_ws, (unsigned)WS_END, FL};
    p.wq = Buf{d_in[3], 512u * 8192u, FL};
    p.wg = Buf{d_in[6], 512u * FF * 2u, FL};
    p.wu = Buf{d_in[7], 512u * FF * 2u, FL};
    p.kc = Buf{d_in[1], 8u * SK * 256u, FL};
    p.wd = Buf{d_in[8], FF * 1024u, FL};
    p.vc = Buf{d_in[2], 8u * SK * 256u, FL};
    p.wo = Buf{d_in[4], 4096u * 1024u, FL};
    p.out = Buf{d_out, 1024u, FL};
    fused_decode<<<256, 448, 0, stream>>>(p);
}
```
